```python
import torch

def _softmax(x, axis=-1):
    return torch.softmax(x, dim=axis)
def _gelu(x):
    return torch.nn.functional.gelu(x, approximate="tanh")
def _silu(x):
    return torch.nn.functional.silu(x)
def _sigmoid(x):
    return torch.sigmoid(x)
def _layernorm(x, axis=-1, eps=1e-5):
    m = x.mean(dim=axis, keepdim=True)
    v = x.var(dim=axis, keepdim=True, unbiased=False)
    return (x - m) / torch.sqrt(v + eps)
def _scatter_add(tgt, idx, src, axis=0):
    return tgt.clone().index_add_(axis, idx, src)
def _segment_sum(src, seg, n):
    out = torch.zeros((n,) + src.shape[1:], dtype=src.dtype)
    return out.index_add_(0, seg, src)
def _pad(x, pw):
    flat = tuple(v for pair in reversed(pw) for v in pair)
    return torch.nn.functional.pad(x, flat)

kernel_name = 'parallel_sum__sliding_local_dilated__dense_activation_squared_relu__prompt'

def setup_inputs():

    _w = (lambda a: (lambda t: t.to(torch.bfloat16) if t.is_floating_point() else t)(torch.as_tensor(a)))
    def _A_setup():
        B, S, D = 1, 2048, 576
        return {
            "x": _w((lambda *s: torch.randn(*s, dtype=torch.bfloat16) * 0.05)(B, S, D)),
            "wqkv": _w((lambda *s: torch.randn(*s, dtype=torch.bfloat16) * 0.1)(D, 3 * D)),
            "wo": _w((lambda *s: torch.randn(*s, dtype=torch.bfloat16) * 0.05)(D, D)),
        }
    _A_in = _A_setup()

    def _B_setup():
        B, S, D, F = 1, 2048, 576, 4608
        return {
            "x": _w((lambda *s: torch.randn(*s, dtype=torch.bfloat16) * 0.05)(B, S, D)),
            "w1": _w((lambda *s: torch.randn(*s, dtype=torch.bfloat16) * 0.05)(D, F)),
            "b1": _w((lambda *s: torch.randn(*s, dtype=torch.bfloat16) * 0.2)(F)),
            "w2": _w((lambda *s: torch.randn(*s, dtype=torch.bfloat16) * 0.05)(F, D)),
            "b2": _w((lambda *s: torch.randn(*s, dtype=torch.bfloat16) * 1.6)(D)),
        }
    _B_in = _B_setup()

    return {
        "A_x": _A_in["x"],
        "A_wqkv": _A_in["wqkv"],
        "A_wo": _A_in["wo"],
        "B_x": _B_in["x"],
        "B_w1": _B_in["w1"],
        "B_b1": _B_in["b1"],
        "B_w2": _B_in["w2"],
        "B_b2": _B_in["b2"],
    }

def reference(A_x, A_wqkv, A_wo, B_x, B_w1, B_b1, B_w2, B_b2):
    def _A_ref(x, wqkv, wo):
        B, S, D = x.shape
        H = 32; Dh = D // H
        W, dil = 8, 2
        qkv = (lambda x, *d: x.permute(*d))((x @ wqkv).reshape(B, S, 3, H, Dh), 2, 0, 3, 1, 4)
        q, k, v = qkv[0], qkv[1], qkv[2]
        scale = Dh ** -0.5
        pos = torch.arange(S)
        off = torch.arange(W) * dil
        idx = pos[:, None] - off[None, :]
        mask = idx >= 0
        idx_c = torch.where(mask, idx, 0).reshape(-1)
        kg = k[:, :, idx_c, :].reshape(B, H, S, W, Dh)
        vg = v[:, :, idx_c, :].reshape(B, H, S, W, Dh)
        logits = torch.einsum("bhsd,bhswd->bhsw", q, kg) * scale
        logits = torch.where(mask[None, None, :, :], logits, -1e9)
        attn = _softmax(logits, axis=-1)
        o = torch.einsum("bhsw,bhswd->bhsd", attn, vg)
        return (lambda x, *d: x.permute(*d))(o, 0, 2, 1, 3).reshape(B, S, D) @ wo

    def _B_ref(x, w1, b1, w2, b2):
        h = torch.einsum("bsd,df->bsf", x, w1) + b1
        zero = torch.zeros_like(h)
        r = torch.where(h > zero, h, zero)
        h = r * r
        out = torch.einsum("bsf,fd->bsd", h, w2) + b2
        return out

    x = A_x
    xn = _layernorm(x, axis=-1)
    return x + _A_ref(xn, A_wqkv, A_wo) + _B_ref(xn, B_w1, B_b1, B_w2, B_b2)
```

```cpp
#include <hip/hip_runtime.h>
#include <hip/hip_bf16.h>
typedef unsigned short u16;
__device__ __forceinline__ float bf2f(u16 v){ return __uint_as_float(((unsigned)v)<<16); }
__device__ __forceinline__ u16 f2bf(float f){ __hip_bfloat16 b = __float2bfloat16(f); return *(u16*)&b; }
__device__ __forceinline__ float rbf(float f){ return bf2f(f2bf(f)); }
constexpr int S=2048, D=576, F=4608, H=32, DH=18;

__global__ void ln_k(const u16* x, u16* xn){
  int r = blockIdx.x; int l = threadIdx.x;
  float v[9]; float s=0;
  for(int i=0;i<9;i++){ v[i]=bf2f(x[r*D+l+64*i]); s+=v[i]; }
  for(int o=32;o;o>>=1) s+=__shfl_xor(s,o);
  float m=s/D; float q=0;
  for(int i=0;i<9;i++){ float d=v[i]-m; q+=d*d; }
  for(int o=32;o;o>>=1) q+=__shfl_xor(q,o);
  float var=rbf(q/D); float mb=rbf(m);
  float sd=rbf(sqrtf(rbf(var+1e-5f)));
  for(int i=0;i<9;i++) xn[r*D+l+64*i]=f2bf(rbf(v[i]-mb)/sd);
}
__global__ void gemm_k(const u16* A, const u16* B, const u16* bias, u16* C, int N, int K, int mode){
  __shared__ float As[16][17], Bs[16][17];
  int tx=threadIdx.x, ty=threadIdx.y; int r=blockIdx.y*16+ty, c=blockIdx.x*16+tx;
  float acc=0;
  for(int k0=0;k0<K;k0+=16){
    As[ty][tx]=bf2f(A[(size_t)r*K+k0+tx]); Bs[ty][tx]=bf2f(B[(size_t)(k0+ty)*N+c]);
    __syncthreads();
    for(int k=0;k<16;k++) acc+=As[ty][k]*Bs[k][tx];
    __syncthreads();
  }
  if(mode==0) C[(size_t)r*N+c]=f2bf(acc);
  else if(mode==1){ float h=rbf(rbf(acc)+bf2f(bias[c])); h=h>0?h:0; C[(size_t)r*N+c]=f2bf(h*h);}
  else { C[(size_t)r*N+c]=f2bf(rbf(acc)+bf2f(bias[c])); }
}
__global__ void attn_k(const u16* qkv, u16* o){
  int idx=blockIdx.x*blockDim.x+threadIdx.x; int s=idx/H, h=idx%H;
  float q[DH]; for(int d=0;d<DH;d++) q[d]=bf2f(qkv[(size_t)s*3*D+h*DH+d]);
  float lg[8]; float mx=-1e30f; const float scale=0.23570226039551584f;
  for(int w=0;w<8;w++){ int t=s-2*w; if(t>=0){ float a=0; for(int d=0;d<DH;d++) a+=q[d]*bf2f(qkv[(size_t)t*3*D+D+h*DH+d]); lg[w]=rbf(rbf(a)*scale);} else lg[w]=rbf(-1e9f); mx=fmaxf(mx,lg[w]); }
  float sum=0; for(int w=0;w<8;w++){ lg[w]=__expf(lg[w]-mx); sum+=lg[w]; }
  for(int w=0;w<8;w++) lg[w]=rbf(lg[w]/sum);
  for(int d=0;d<DH;d++){ float a=0; for(int w=0;w<8;w++){ int t=s-2*w; if(t<0) t=0; a+=lg[w]*bf2f(qkv[(size_t)t*3*D+2*D+h*DH+d]); } o[(size_t)s*D+h*DH+d]=f2bf(a);}
}
__global__ void fin_k(const u16* x,const u16* a,const u16* b,u16* out){ int i=blockIdx.x*blockDim.x+threadIdx.x; out[i]=f2bf(rbf(bf2f(x[i])+bf2f(a[i]))+bf2f(b[i])); }

extern "C" void kernel_launch(void* const* d_in, const int* in_sizes, int n_in, void* d_out, int out_size, void* d_ws, size_t ws_size, hipStream_t stream){
  const u16* x=(const u16*)d_in[0]; const u16* wqkv=(const u16*)d_in[1]; const u16* wo=(const u16*)d_in[2];
  const u16* w1=(const u16*)d_in[4]; const u16* b1=(const u16*)d_in[5]; const u16* w2=(const u16*)d_in[6]; const u16* b2=(const u16*)d_in[7];
  u16* ws=(u16*)d_ws; u16* xn=ws; u16* qkv=xn+S*D; u16* o=qkv+S*3*D; u16* h=o+S*D; u16* a=h+S*F; u16* b=a+S*D;
  ln_k<<<S,64,0,stream>>>(x,xn);
  gemm_k<<<dim3(3*D/16,S/16),dim3(16,16),0,stream>>>(xn,wqkv,nullptr,qkv,3*D,D,0);
  attn_k<<<S*H/256,256,0,stream>>>(qkv,o);
  gemm_k<<<dim3(D/16,S/16),dim3(16,16),0,stream>>>(o,wo,nullptr,a,D,D,0);
  gemm_k<<<dim3(F/16,S/16),dim3(16,16),0,stream>>>(xn,w1,b1,h,F,D,1);
  gemm_k<<<dim3(D/16,S/16),dim3(16,16),0,stream>>>(h,w2,b2,b,D,F,2);
  fin_k<<<S*D/256,256,0,stream>>>(x,a,b,(u16*)d_out);
}
```

```cpp
#include <hip/hip_runtime.h>
#include <hip/hip_bf16.h>
#include <utility>
typedef unsigned short u16; typedef unsigned u32; typedef unsigned long long u64;
typedef __attribute__((ext_vector_type(8))) __bf16 bf16x8;
typedef __attribute__((ext_vector_type(2))) __bf16 bf16x2;
typedef __attribute__((ext_vector_type(4))) short s16x4;
typedef __attribute__((ext_vector_type(2))) short s16x2;
typedef __attribute__((ext_vector_type(4))) float f32x4;
typedef __attribute__((ext_vector_type(2))) float f32x2;
typedef __attribute__((ext_vector_type(4))) u32 u32x4;
typedef __attribute__((ext_vector_type(2))) u32 u32x2;
#define LAS __attribute__((address_space(3)))
#define GAS __attribute__((address_space(1)))
#define DEVI __device__ __forceinline__
#define RLX_AGENT __ATOMIC_RELAXED, __HIP_MEMORY_SCOPE_AGENT
#define SB __builtin_amdgcn_sched_barrier(0)
#define BAR asm volatile("s_barrier" ::: "memory")
#define LBAR asm volatile("s_waitcnt lgkmcnt(0)\n\ts_barrier" ::: "memory")
#define VM0_SEEN __builtin_amdgcn_s_waitcnt(0x0F70)
#define IC(x) std::integral_constant<int,(x)>{}
#define PIN(x) asm volatile("" : "+v"(x))
#define MFMA(a,b,c) __builtin_amdgcn_mfma_f32_16x16x32_bf16(a,b,c,0,0,0)

constexpr int NKS=41;
constexpr size_t HF_OFF = 0, HF_BYTES = (size_t)64*NKS*8192;
constexpr size_t XCH_OFF = HF_OFF + HF_BYTES, XCH_BYTES = (size_t)64*4*36864;
constexpr size_t HALO_OFF = XCH_OFF + XCH_BYTES, HALO_BYTES = (size_t)64*8192;
constexpr size_t SYN_OFF = HALO_OFF + HALO_BYTES;
constexpr int LDS_BYTES = 163840, TOP = 159744;
constexpr int SCR = TOP+1792, BRAW = TOP+2048, EPO = TOP+3072, STA = TOP+3328;
constexpr int BUF0 = 86016, BUF1 = 12288;
constexpr int ROWB = 864, PARB = 71*ROWB;

DEVI u32 pk2(float a, float b){ f32x2 p={a,b}; bf16x2 q=__builtin_convertvector(p,bf16x2); return __builtin_bit_cast(u32,q); }
DEVI float lo2f(u32 v){ return __uint_as_float(v<<16); }
DEVI float hi2f(u32 v){ return __uint_as_float(v&0xffff0000u); }
DEVI float rbf(float f){ return lo2f(pk2(f,f)); }
DEVI void glds16(u32 lds, const void* sbase, u32 voff){
  asm volatile("s_mov_b32 m0, %0\n\ts_nop 0\n\tglobal_load_lds_dwordx4 %1, %2" :: "s"(lds), "v"(voff), "s"(sbase) : "memory");
}
DEVI void setm0(u32 lds){ asm volatile("s_mov_b32 m0, %0\n\ts_nop 0" :: "s"(lds) : "memory"); }
template<int OFF, bool DEV> DEVI void gl(const void* sbase, u32 voff){
  if constexpr (DEV) asm volatile("global_load_lds_dwordx4 %0, %1 offset:%2 sc1" :: "v"(voff), "s"(sbase), "n"(OFF) : "memory");
  else asm volatile("global_load_lds_dwordx4 %0, %1 offset:%2" :: "v"(voff), "s"(sbase), "n"(OFF) : "memory");
}
DEVI void kstep4(u32 lds, const char* sbase, const u32 (&v)[4]){
  asm volatile("s_mov_b32 m0, %0\n\ts_nop 0\n\tglobal_load_lds_dwordx4 %2, %1\n\tglobal_load_lds_dwordx4 %3, %1 offset:1024\n\t"
               "global_load_lds_dwordx4 %4, %1 offset:2048\n\tglobal_load_lds_dwordx4 %5, %1 offset:3072"
               :: "s"(lds), "s"(sbase), "v"(v[0]), "v"(v[1]), "v"(v[2]), "v"(v[3]) : "memory");
}
DEVI void kpair(u32 lds, const char* sbase, u32 va, u32 vb){
  asm volatile("s_mov_b32 m0, %0\n\ts_nop 0\n\tglobal_load_lds_dwordx4 %2, %1\n\tglobal_load_lds_dwordx4 %3, %1 offset:1024"
               :: "s"(lds), "s"(sbase), "v"(va), "v"(vb) : "memory");
}
DEVI void glds4(u32 lds, const void* sbase, u32 voff){
  asm volatile("s_mov_b32 m0, %0\n\ts_nop 0\n\tglobal_load_lds_dword %1, %2" :: "s"(lds), "v"(voff), "s"(sbase) : "memory");
}
DEVI void touch(u32 lds, const char* p){
  asm volatile("s_mov_b32 m0, %0\n\ts_nop 0\n\tglobal_load_lds_dword %1, off" :: "s"(lds), "v"(p) : "memory");
}
template<int N> DEVI void wait_vm(){ asm volatile("s_waitcnt vmcnt(%0)" :: "n"(N) : "memory"); }
DEVI bf16x8 ldsb128(u32 a){ return *(LAS const bf16x8*)(size_t)a; }
DEVI s16x4 ldstr(u32 a){ return __builtin_amdgcn_ds_read_tr16_b64_v4i16((LAS s16x4*)(size_t)a); }
DEVI bf16x8 cat(s16x4 a, s16x4 b){ return __builtin_bit_cast(bf16x8, __builtin_shufflevector(a,b,0,1,2,3,4,5,6,7)); }
DEVI void mfma_acc(f32x4& acc, bf16x8 a, bf16x8 b){ asm volatile("v_mfma_f32_16x16x32_bf16 %0, %1, %2, %0" : "+v"(acc) : "v"(a), "v"(b)); }
DEVI void mfma_new(f32x4& acc, bf16x8 a, bf16x8 b, f32x4 c){ asm volatile("v_mfma_f32_16x16x32_bf16 %0, %1, %2, %3" : "+v"(acc) : "v"(a), "v"(b), "v"(c)); }
DEVI void mfma_zero(f32x4& acc, bf16x8 a, bf16x8 b){ asm volatile("v_mfma_f32_16x16x32_bf16 %0, %1, %2, 0" : "+v"(acc) : "v"(a), "v"(b)); }
DEVI void mfma_set(f32x4& acc, bf16x8 a, bf16x8 b){ asm volatile("v_mfma_f32_16x16x32_bf16 %0, %1, %2, 0" : "=&v"(acc) : "v"(a), "v"(b)); }
#define NOP8 do{ SB; asm volatile("s_nop 7\n\ts_nop 1"); SB; }while(0)
DEVI void sw16(float& a, float& b){ auto r = __builtin_amdgcn_permlane16_swap(__float_as_uint(a), __float_as_uint(b), false, false); a=__uint_as_float(r[0]); b=__uint_as_float(r[1]); }
DEVI void sw32(float& a, float& b){ auto r = __builtin_amdgcn_permlane32_swap(__float_as_uint(a), __float_as_uint(b), false, false); a=__uint_as_float(r[0]); b=__uint_as_float(r[1]); }
DEVI float xsum(float v){
  auto a = __builtin_amdgcn_permlane16_swap(__float_as_uint(v), __float_as_uint(v), false, false);
  v = __uint_as_float(a[0]) + __uint_as_float(a[1]);
  auto b = __builtin_amdgcn_permlane32_swap(__float_as_uint(v), __float_as_uint(v), false, false);
  return __uint_as_float(b[0]) + __uint_as_float(b[1]);
}

template<class F, int... I> DEVI void sfor_(F&& f, std::integer_sequence<int,I...>){ (f(std::integral_constant<int,I>{}), ...); }
template<int N, class F> DEVI void sfor(F&& f){ sfor_(f, std::make_integer_sequence<int,N>{}); }

struct Ctx { const char *x,*wqkv,*wo,*w1,*b1,*w2,*b2; char *out,*ws; int r,i,u,w,lane; const char* code; u32 clen; };
DEVI GAS u64* synw(const Ctx& c, int r, int line){ return (GAS u64*)(c.ws + SYN_OFF + (size_t)r*8192 + line*128); }
DEVI bool flag_ok(u64 v, u64 tag, u32 cnt){ return (v>>3)==tag && ((u32)v&7u) >= cnt; }
DEVI void need(GAS u64* p, u64& v, bool act, u64 tag, u32 cnt){
  u32 spins = 0;
  while(!__all(!act || flag_ok(v,tag,cnt))){
    if (++spins > (1u<<19)) break;
    __builtin_amdgcn_s_sleep(1);
    v = __hip_atomic_load(p, RLX_AGENT);
  }
  VM0_SEEN;
}

DEVI u32 p1_chunk(int lane){ return (lane&7) ^ ((lane>>3)&6); }
DEVI void p1_vB(u32 (&v)[4], int lane){
  #pragma unroll
  for(int z=0;z<4;z++) v[z] = ((lane>>3)+8*(z^2))*9216 + p1_chunk(lane)*16 + 4096 - 1024*z;
}
DEVI void a_blocks(u32 (&t)[7], int i){
  t[0] = i==0? 0x03020100u : i==1? 0x06050403u : i==2? 0x05040302u : 0x04030201u;
  t[1] = i==0? 0x07060504u : i==1? 0x0f0e0d0cu : i==2? 0x0e0d0c0bu : 0x08070605u;
  t[2] = i==0? 0x0c0b0a09u : i==1? 0x11100807u : i==2? 0x0a090100u : 0x0d0c0b0au;
  t[3] = i==0? 0x100f0e0du : i==1? 0x19020100u : i==2? 0x12080706u : 0x11100f0eu;
  t[4] = i==0? 0x1a1a1108u : i==1? 0x1a0b0a09u : i==2? 0x1311100fu : 0x12120900u;
  t[5] = i==0? 0x15141312u : i==1? 0x18171615u : i==2? 0x17161514u : 0x16151413u;
  t[6] = i==0? 0x19181716u : i==1? 0x14141312u : i==2? 0x1a1a1918u : 0x1a191817u;
}
DEVI u32 a_col(int lane, u32 tb){
  const int ch = p1_chunk(lane), id = (tb >> (8*(ch>>1))) & 31;
  return id*32 + (ch&1)*16 + (id>=18? -576 : (id>=9? 2304-288 : 1152));
}
DEVI void p1_vA(u32 (&v)[4], int lane, u32 tb, int i){
  const u32 co = a_col(lane, tb);
  #pragma unroll
  for(int z=0;z<4;z++) v[z] = ((lane>>3)+8*(z^2))*3456 + 288*i + 4096 - 1024*z + co;
}

constexpr int NB = 12, SLOT = 8192 + NB*1024, RS = NB*32, NS = 7;
constexpr int p2low(int j){ return j*3+2 > NS-1 ? NS-1 : j*3+2; }

DEVI void p2_base(u32 (&b3)[3], const Ctx& c){
  int lane = c.lane; PIN(lane);
  #pragma unroll
  for(int z=0;z<3;z++){
    const int ci = z*64+lane, rho = ci/24, ph = ci-rho*24;
    b3[z] = rho*1152 + ((ph ^ (rho&6))<<4) + 4096; PIN(b3[z]);
  }
}
template<int L> DEVI void p2_voff(u32 (&voff)[7], u32 (&vlast)[7], const u32 (&b3)[3], const Ctx& c){
  #pragma unroll
  for(int z=0;z<7;z++){
    const int q = L*7+z, k = (q-8)/3;
    if (q<8) voff[z] = vlast[z] = (q^c.i)*1024 + c.lane*16 + 4096 - (z&3)*1024;
    else { voff[z] = b3[(q-8)%3] + k*8*1152 - (z&3)*1024; vlast[z] = b3[(q-8)%3] + (72*(k>>1)+64)*1152 - (z&3)*1024; }
  }
}
template<int L> struct P2L {
  static constexpr int I0 = 7*L, N = L<2? 7:6;
  const char* hp; const char* wp; const char* woc; u32 slot;
  DEVI void init(const Ctx& c){
    slot = TOP - SLOT + I0*1024;
    hp = c.ws + HF_OFF + (size_t)(c.r*4+c.i)*NKS*8192 - 4096;
    wp = c.w2 + (size_t)c.i*1152*1152 + c.u*384 - 4096; woc = c.wo + (size_t)c.i*144*1152 + c.u*384 - 4096;
  }
  DEVI void step(const u32 (&voff)[7]){
    sfor<N>([&](auto z_){ constexpr int z=decltype(z_)::value;
      if constexpr ((z&3)==0) setm0(slot + z*1024);
      if constexpr (I0+z<8) gl<(z&3)*1024,true>(hp, voff[z]); else gl<(z&3)*1024,false>(wp, voff[z]); });
    hp += 8192; wp += 32*1152; slot -= SLOT;
    if (slot == (u32)(TOP - (NS+1)*SLOT + I0*1024)) slot += NS*SLOT;
  }
  DEVI void steps(const u32 (&voff)[7], int n){
    #pragma unroll 1
    for(int k=0;k<n;k++) step(voff);
  }
  DEVI void run(const u32 (&voff)[7], const u32 (&vlast)[7]){
    #pragma unroll 1
    for(int b=0;b<17;b++){
      wait_vm<2*N>(); BAR;
      step(voff);
      if (b==15) wp = woc;
      if (b==16) wp += 8*1152;
      step(voff);
    }
    wait_vm<2*N>(); BAR; step(voff); wp = woc; step(vlast);
    wait_vm<2*N>(); BAR;
    wait_vm<0>(); BAR;
    BAR;
  }
};

DEVI auto xch_rsrc(const Ctx& c, int u){ return __builtin_amdgcn_make_buffer_rsrc(c.ws + XCH_OFF + (size_t)(c.r*4+u)*4*36864, 0, 4*36864, 0x00020000); }

DEVI void p2_sync(const Ctx& c, u64 tag){
  const int l7 = c.lane&7;
  GAS u64* pf = synw(c, c.r, 8 + (l7<4 ? c.i*4 + l7 : (l7-4)*4 + 3));
  GAS u64* mine = synw(c,c.r,8+c.i*4+c.u);
  u64 v = 0;
  sfor<21>([&](auto s_){ constexpr int s=decltype(s_)::value;
    if constexpr (s==4) need(pf, v, l7<3, tag, 4);
    if constexpr (s==9) need(pf, v, l7<3, tag, 6);
    if constexpr (s==15) need(pf, v, l7==3, tag, 5);
    if constexpr (s==16) need(pf, v, l7==3, tag, 6);
    if constexpr (s==20) need(pf, v, l7>=4, tag, 7);
    if constexpr (s==2 || s==7 || s==13 || s==18) v = __hip_atomic_load(pf, RLX_AGENT);
    BAR;
    if constexpr (s==4){ if (c.lane==0) __hip_atomic_store(mine, (tag<<3)|6, RLX_AGENT); }
  });
  VM0_SEEN;
}

template<class PRE>
DEVI void p2_compute(const Ctx& c, PRE pre){
  const int lane=c.lane, t=c.w, g=lane>>4, li=lane&15, q=(lane&15)>>2, p=lane&3;
  u32 bh[3], bw[3][4];
  const u32 F = ((q>>1)&1) | ((g&1)<<1);
  const int th = t>>1, cf = t&1;
  #pragma unroll
  for(int j=0;j<3;j++){
    const u32 lowb = TOP - (p2low(j)+1)*SLOT, pb = lowb + 8192 + (4*g+q)*RS + 8*p;
    bh[j] = lowb + th*4096 + lane*16; PIN(bh[j]);
    #pragma unroll
    for(int z=0;z<4;z++){ bw[j][z] = pb + 32*(((6*cf+z)&3)^F) + 128*((6*cf+z)>>2); PIN(bw[j][z]); }
  }
  constexpr int NW = 6, R2 = 6, LD2 = 3;
  s16x4 lo[R2], hi[R2]; bf16x8 h[2][4];
  auto rdw = [&](auto U_, auto half_){
    constexpr int U=decltype(U_)::value % (14*NW), k=(U/NW)%NS, n=U%NW, j=k/3, o=(p2low(j)-k)*SLOT + (n>>2)*128;
    if constexpr (decltype(half_)::value==0) lo[U%R2] = ldstr(bw[j][n&3]+o); else hi[U%R2] = ldstr(bw[j][n&3]+o+16*RS);
  };
  auto rdh = [&](auto s_, auto e_){
    constexpr int s=decltype(s_)::value % 14, k=s%NS, j=k/3, e=decltype(e_)::value;
    h[s&1][e] = ldsb128(bh[j] + (p2low(j)-k)*SLOT + e*1024);
  };
  rdw(IC(0),IC(0)); rdw(IC(0),IC(1));
  sfor<4>([&](auto e_){ rdh(IC(0),e_); });
  sfor<LD2-1>([&](auto U_){ rdw(IC(decltype(U_)::value+1),IC(0)); rdw(IC(decltype(U_)::value+1),IC(1)); });
  f32x4 acc[4][NW];
  auto unit = [&](auto U_, auto mode_){ constexpr int U=decltype(U_)::value, k=U/NW, n=U%NW, mode = decltype(mode_)::value, e0 = mode==2;
    constexpr bool rdn = !(mode==2 && U+LD2 >= 13*NW);
    const bf16x8 a = cat(lo[U%R2], hi[U%R2]);
    sfor<4-e0>([&](auto z_){ constexpr int z=decltype(z_)::value, e = (n&1) ? 3-z : z+e0;
      if constexpr (mode==1) mfma_set(acc[e][n], a, h[k&1][e]); else mfma_acc(acc[e][n], a, h[k&1][e]);
      SB;
      if constexpr (rdn){
        if constexpr (z==0 && n==NW-LD2 && (k&1)==0) BAR;
        if constexpr (z<2) rdw(IC(U+LD2),z_);
        if constexpr (mode!=2 && z>=2 && n>=NW-LD2 && n<NW-LD2+2) rdh(IC(k+1),IC(2*(n-NW+LD2)+z-2));
        if constexpr (mode==2 && n==NW-LD2 && z==2) rdh(IC(k+1),IC(1));
        if constexpr (mode==2 && n==NW-LD2+1 && z!=1) rdh(IC(k+1),IC(2+z/2));
      } else if constexpr (z<2) rdw(IC(U+LD2-7*NW),z_);
      if constexpr (mode==0 && k==8 && n==NW-LD2-1 && z==3) wait_vm<0>();
      if constexpr (mode==1){ if constexpr (z<2) pre(IC(6*n+z)); else { pre(IC(6*n+2*z-2)); pre(IC(6*n+2*z-1)); } }
      SB;
    });
  };
  sfor<NW>([&](auto U_){ unit(U_, IC(1)); });
  #pragma unroll 1
  for(int it=0;it<3;it++){
    sfor<5*NW>([&](auto U_){ unit(IC(decltype(U_)::value + NW), IC(0)); });
    int more = 2-it; asm volatile("" : "+s"(more));
    if (more) sfor<9*NW>([&](auto U_){ unit(IC(decltype(U_)::value + 6*NW), IC(0)); });
  }
  sfor<7*NW>([&](auto U_){ unit(IC(decltype(U_)::value + 6*NW), IC(2)); });
  auto rs = xch_rsrc(c, c.u);
  u32x4 sv;
  auto send = [&](auto o_){ constexpr int o=decltype(o_)::value, un=o/9, z=o%9, k=un/3, d=un%3+1, n=2*k;
    {
      if constexpr (z<4){ u32 tv = pk2(acc[d][n+(z>>1)][(z&1)*2], acc[d][n+(z>>1)][(z&1)*2+1]); PIN(tv); sv[z] = tv; }
      else if constexpr (z<8){ u32 tv = sv[z-4]; tv = tv < ~1u ? tv : ~1u; PIN(tv); sv[z-4] = tv; }
      else __builtin_amdgcn_raw_buffer_store_b128(sv, rs, lane*16 + (d-1)*1024, ((c.i^d)*4+t)*9216 + k*3072, 16);
    } };
  rdw(IC(6*NW+3),IC(0)); rdw(IC(6*NW+3),IC(1)); rdw(IC(6*NW+4),IC(0)); rdw(IC(6*NW+4),IC(1));
  SB;
  BAR;
  sfor<81>(send);
  sfor<7*NW>([&](auto U_){ constexpr int V=decltype(U_)::value, U=V+6*NW, k=U/NW, n=U%NW;
    SB;
    mfma_acc(acc[0][n], cat(lo[U%R2], hi[U%R2]), h[k&1][0]);
    SB;
    if constexpr (V+5 < 7*NW){ rdw(IC(U+5),IC(0)); rdw(IC(U+5),IC(1)); }
    if constexpr (n==0 && k<12) rdh(IC(k+1),IC(0));
  });
  NOP8;
  auto ro = __builtin_amdgcn_make_buffer_rsrc(c.out, 0, 2048*1152, 0x00020000);
  const u32 cb = (g&1)*32 + (g>>1)*16, vo = (c.r*128 + th*64 + (c.i>>1)*32 + 2*li + (c.i&1))*1152 + cb, so = c.u*384 + cf*192;
  u32x4 xv[3], bv[3], pp[3][3];
  #pragma unroll
  for(int k=0;k<3;k++){ xv[k] = *(LAS const u32x4*)(size_t)(t*3072 + k*1024 + lane*16); bv[k] = *(LAS const u32x4*)(size_t)(BRAW + cf*192 + cb + k*64); }
  bf16x8 sp[2];
  { const u32 one = 0x3f80u << ((li&1)*16);
    const u32 a = g==(li>>2) ? one : 0u, d0 = (li&2)? 0u:a, d1 = (li&2)? a:0u;
    sp[0] = __builtin_bit_cast(bf16x8, u32x4{d0,d1,0u,0u}); sp[1] = __builtin_bit_cast(bf16x8, u32x4{0u,0u,d0,d1});
    #pragma unroll
    for(int z=0;z<2;z++){
      const u32 b = g==(li>>3)*2+z ? one : 0u; const int dw = (li>>1)&3;
      const bf16x8 sx = __builtin_bit_cast(bf16x8, u32x4{dw==0? b:0u, dw==1? b:0u, dw==2? b:0u, dw==3? b:0u});
      #pragma unroll
      for(int k=0;k<3;k++){ acc[0][2*k+z] = MFMA(sx, __builtin_bit_cast(bf16x8,xv[k]), acc[0][2*k+z]); acc[0][2*k+z] = MFMA(sx, __builtin_bit_cast(bf16x8,bv[k]), acc[0][2*k+z]); }
    }
    #pragma unroll
    for(int n=0;n<NW;n++) PIN(acc[0][n]);
    PIN(sp[0]); PIN(sp[1]);
  }
  for(u32 spins=0; spins<(1u<<19); spins++){
    const u32 w9 = __builtin_amdgcn_raw_buffer_load_b32(rs, (lane<9? lane:8)*1024 + 1020, (c.i*4+t)*9216, 16);
    if (__all(w9 != ~0u)) break;
    __builtin_amdgcn_s_sleep(2);
    asm volatile("" ::: "memory");
  }
  for(u32 spins=0; spins<(1u<<19); spins++){
    u32 mx = 0;
    #pragma unroll
    for(int k=0;k<3;k++){
      #pragma unroll
      for(int z=0;z<3;z++) pp[k][z] = __builtin_amdgcn_raw_buffer_load_b128(rs, lane*16 + z*1024, (c.i*4+t)*9216 + k*3072, 16);
    }
    #pragma unroll
    for(int k=0;k<3;k++){
      const u32x4 m4 = __builtin_elementwise_max(__builtin_elementwise_max(pp[k][0],pp[k][1]),pp[k][2]);
      mx = max(max(mx, max(m4[0],m4[1])), max(m4[2],m4[3]));
    }
    if (__all(mx != ~0u)) break;
    __builtin_amdgcn_s_sleep(2);
    asm volatile("" ::: "memory");
  }
  #pragma unroll
  for(int k=0;k<3;k++){
    #pragma unroll
    for(int z=0;z<3;z++){ const bf16x8 pf = __builtin_bit_cast(bf16x8, pp[k][z]); acc[0][2*k] = MFMA(sp[0], pf, acc[0][2*k]); acc[0][2*k+1] = MFMA(sp[1], pf, acc[0][2*k+1]); }
  }
  #pragma unroll
  for(int k=0;k<3;k++){
    const f32x4 a = acc[0][2*k], b = acc[0][2*k+1];
    auto r0 = __builtin_amdgcn_permlane16_swap(pk2(a[0],a[1]), pk2(b[0],b[1]), false, false);
    auto r1 = __builtin_amdgcn_permlane16_swap(pk2(a[2],a[3]), pk2(b[2],b[3]), false, false);
    __builtin_amdgcn_raw_buffer_store_b128(u32x4{r0[0], r1[0], r0[1], r1[1]}, ro, vo + k*64, so, 17);
  }
  if (c.i==0 && c.u==0 && c.w==0 && lane==0) __hip_atomic_store(synw(c,c.r,0), *(LAS const u64*)(size_t)EPO + 1, RLX_AGENT);
  VM0_SEEN;
}

DEVI float fmx(float a, float b){ return __builtin_elementwise_maximum(a,b); }
template<bool MAX> DEVI float red4(float (&v)[4]){
  sw16(v[0],v[1]); sw16(v[2],v[3]);
  float x = MAX? fmx(v[0],v[1]) : v[0]+v[1], y = MAX? fmx(v[2],v[3]) : v[2]+v[3];
  sw32(x,y);
  return MAX? fmx(x,y) : x+y;
}
DEVI void bcast4(float r, float (&v)[4]){
  float x=r, y=r; sw32(x,y);
  v[0]=x; v[1]=x; sw16(v[0],v[1]); v[2]=y; v[3]=y; sw16(v[2],v[3]);
}
DEVI void att_tables(int lane){
  const int g=lane>>4, li=lane&15;
  f32x4 c0, c1; u32x2 m0;
  #pragma unroll
  for(int r=0;r<4;r++){ const int cc=4*g+r; c0[r] = (cc>=li && cc<=li+7)? 0.f : -1e30f; c1[r] = (cc+9<=li)? 0.f : -1e30f; }
  #pragma unroll
  for(int d=0;d<2;d++) m0[d] = ((4*g+2*d>=li)? 0xffffu:0u) | ((4*g+2*d+1>=li)? 0xffff0000u:0u);
  *(LAS f32x4*)(size_t)(TOP + lane*16) = c0; *(LAS f32x4*)(size_t)(BRAW + lane*16) = c1; *(LAS u32x2*)(size_t)(TOP + 1024 + lane*8) = m0;
}
DEVI void attention(const Ctx& c, int tb){
  const int lane=c.lane, bt=tb>>1, par=tb&1, g=lane>>4, li=lane&15, q=li>>2, p=lane&3;
  const u32 qa = par*PARB + (7+16*bt+li)*ROWB + 576 + 16*g;
  u32 ka[2], va[2], kx[2], vx[2];
  const u32 qx = ((7+li)&4)*4;
  #pragma unroll
  for(int z=0;z<2;z++){
    int rk = 16*bt+16*z+li; rk = rk>70?70:rk; ka[z] = par*PARB + rk*ROWB + 16*g; kx[z] = (rk&4)*4;
    int rv = 16*bt+16*z+4*g+q; rv = rv>70?70:rv; va[z] = par*PARB + rv*ROWB + 288 + 8*p; vx[z] = (rv&4)*4;
  }
  f32x4 c0 = *(LAS const f32x4*)(size_t)(TOP + lane*16); const f32x4 c1 = *(LAS const f32x4*)(size_t)(BRAW + lane*16);
  const u32x2 m0 = *(LAS const u32x2*)(size_t)(TOP + 1024 + lane*8);
  if (c.r==0 && bt==0){
    #pragma unroll
    for(int r=0;r<4;r++) c0[r] = 4*g+r<7 ? -1e30f : c0[r];
  }
  auto rs = __builtin_amdgcn_make_buffer_rsrc(c.ws + HF_OFF + (size_t)(c.r*4+c.i)*NKS*8192, 0, NKS*8192, 0x00020000);
  const float sc = 0.23570226039551584f*1.4426950408889634f;
  u32 last[4];
  #pragma unroll
  for(int hf=0;hf<2;hf++){
    const u32 ho = 144*hf, q0 = (qa+ho)^qx, k0 = (ka[0]+ho)^kx[0], k1 = (ka[1]+ho)^kx[1], v0 = (va[0]+ho)^vx[0], v1 = (va[1]+ho)^vx[1];
    float l[4][4], t4[4], b4[4];
    bf16x8 vf[5];
    #pragma unroll
    for(int hl=0;hl<4;hl++){
      u32x4 qv = __builtin_bit_cast(u32x4, ldsb128(q0+32*hl));
      #pragma unroll
      for(int d=0;d<4;d++) qv[d] = ((u32)(4*g+d-hl) < 9u) ? qv[d] : 0u;
      const bf16x8 qf = __builtin_bit_cast(bf16x8, qv);
      f32x4 s0 = MFMA(ldsb128(k0+32*hl), qf, c0), s1 = MFMA(ldsb128(k1+32*hl), qf, c1);
      #pragma unroll
      for(int r=0;r<4;r++) l[hl][r] = fmx(s0[r], s1[r]);
      t4[hl] = fmx(fmx(l[hl][0],l[hl][1]), fmx(l[hl][2],l[hl][3]));
    }
    #pragma unroll
    for(int om=0;om<5;om++) vf[om] = cat(ldstr(v0+32*om), ldstr(v1+32*om));
    bcast4(red4<true>(t4)*(-sc), b4);
    #pragma unroll
    for(int hl=0;hl<4;hl++){
      #pragma unroll
      for(int r=0;r<4;r++) l[hl][r] = __builtin_amdgcn_exp2f(__builtin_fmaf(l[hl][r], sc, b4[hl]));
      t4[hl] = (l[hl][0]+l[hl][1])+(l[hl][2]+l[hl][3]);
    }
    bcast4(__builtin_amdgcn_rcpf(red4<false>(t4)), b4);
    bf16x8 P[4];
    #pragma unroll
    for(int hl=0;hl<4;hl++){
      const u32 a = pk2(l[hl][0]*b4[hl], l[hl][1]*b4[hl]), b = pk2(l[hl][2]*b4[hl], l[hl][3]*b4[hl]);
      P[hl] = __builtin_bit_cast(bf16x8, u32x4{a&m0[0], b&m0[1], a&~m0[0], b&~m0[1]});
    }
    u32 ow[5][2];
    #pragma unroll
    for(int om=0;om<5;om++){
      const f32x4 z4 = {0,0,0,0};
      u32 a[2]={0,0}, b[2]={0,0};
      if (om>0){ f32x4 o = MFMA(vf[om], P[om>0?om-1:0], z4); a[0]=pk2(o[0],o[1]); a[1]=pk2(o[2],o[3]); }
      if (om<4){ f32x4 o = MFMA(vf[om], P[om<4?om:0], z4); b[0]=pk2(o[0],o[1]); b[1]=pk2(o[2],o[3]); }
      #pragma unroll
      for(int d=0;d<2;d++) ow[om][d] = (2*g+d < om) ? a[d] : b[d];
    }
    __builtin_amdgcn_raw_buffer_store_b128(u32x4{ow[0][0],ow[0][1],ow[1][0],ow[1][1]}, rs, lane*16, ((36+2*hf)*8+tb)*1024, 16);
    __builtin_amdgcn_raw_buffer_store_b128(u32x4{ow[2][0],ow[2][1],ow[3][0],ow[3][1]}, rs, lane*16, ((37+2*hf)*8+tb)*1024, 16);
    last[2*hf]=ow[4][0]; last[2*hf+1]=ow[4][1];
    SB;
  }
  __builtin_amdgcn_raw_buffer_store_b128(u32x4{last[0],last[1],last[2],last[3]}, rs, lane*16, (40*8+tb)*1024, 16);
}

constexpr int RING = 6, LEADB = 5;
DEVI void computeB(const Ctx& c, bf16x8 (&xf)[2][18]){
  const int lane=c.lane, t=c.w, g=lane>>4, q=(lane&15)>>2, p=lane&3;
  const u32 F = ((q>>1)&1) | ((g&1)<<1);
  const u32 lp = (4*g+q)*128 + 32*F + 8*p;
  auto rs = __builtin_amdgcn_make_buffer_rsrc(c.ws + HF_OFF + (size_t)(c.r*4+c.i)*NKS*8192, 0, NKS*8192, 0x00020000);
  s16x4 lo[RING], hi[RING];
  f32x4 acc[2][2][2], bias[2];
  u32x4 pkv = {0,0,0,0};
  #pragma unroll
  for(int a=0;a<2;a++) for(int b=0;b<2;b++) for(int e=0;e<2;e++) asm volatile("" : "=v"(acc[a][b][e]));
  u32 a0 = BUF0 + 36864 + lp, a1 = a0 ^ 32;
  PIN(a0); PIN(a1);
  u32 ba = TOP + 16*g; PIN(ba);
  wait_vm<0>(); BAR;
  bias[0] = *(LAS const f32x4*)(size_t)(ba); bias[1] = *(LAS const f32x4*)(size_t)(ba+64);
  #pragma unroll
  for(int m=0;m<LEADB;m++){ const u32 b=(m&1)?a1:a0; const int s=m>>1; lo[m]=ldstr(b+(8-s)*4096+2048); hi[m]=ldstr(b+(8-s)*4096); }
  int dbuf = BUF1-BUF0;
  u32 soff = 1u<<30;
  auto epi = [&](auto hq_, auto o_){
    constexpr int hq = decltype(hq_)::value, o = decltype(o_)::value, e = o/17, k = o%17;
    if constexpr (k<8){ float v = acc[hq][k>>2][e][k&3]; v = v*__builtin_fabsf(v); PIN(v); acc[hq][k>>2][e][k&3] = v; }
    else if constexpr (k<12){ constexpr int z=k-8; u32 tv = pk2(acc[hq][z>>1][e][(z&1)*2], acc[hq][z>>1][e][(z&1)*2+1]); PIN(tv); pkv[z] = tv; }
    else if constexpr (k<16){ constexpr int z=k-12; const u32 tv = pkv[z]; pkv[z] = __builtin_bit_cast(u32, __builtin_elementwise_max(__builtin_bit_cast(s16x2,tv), s16x2{0,0})); }
    else __builtin_amdgcn_raw_buffer_store_b128(pkv, rs, lane*16, soff + e*1024, 16);
  };
  #pragma unroll 1
  for(int P=0;P<6;P++){
    sfor<72>([&](auto n_){
      constexpr int n=decltype(n_)::value, hp=n/36, s=(n%36)>>1, bb=n&1, m=(n+LEADB)%72, ms=(m%36)>>1;
      const bf16x8 a = cat(lo[n%RING], hi[n%RING]);
      constexpr int e0 = bb, e1 = 1-e0;
      if constexpr (s==0) mfma_new(acc[hp][bb][e0], a, xf[e0][s], bias[bb]); else mfma_acc(acc[hp][bb][e0], a, xf[e0][s]);
      SB;
      if constexpr (n==35) wait_vm<4>();
      if constexpr (n==18 || n==35 || n==54 || n==71) BAR;
      lo[(n+LEADB)%RING] = ldstr(((m&1)?a1:a0) + (8-ms%9)*4096 + 2048);
      if constexpr (n%36>=1 && n%36<35) epi(std::integral_constant<int,hp^1>{}, std::integral_constant<int,n%36-1>{});
      if constexpr (n%36==0){ ba += 128; PIN(ba); }
      SB;
      if constexpr (s==0) mfma_new(acc[hp][bb][e1], a, xf[e1][s], bias[bb]); else mfma_acc(acc[hp][bb][e1], a, xf[e1][s]);
      SB;
      hi[(n+LEADB)%RING] = ldstr(((m&1)?a1:a0) + (8-ms%9)*4096);
      if constexpr (m%18 >= 16){
        u32& ab = (m&1)? a1 : a0;
        if constexpr (m<18 || (m>=36 && m<54)) ab -= 36864;
        else if constexpr (m<36) ab = (ab + 36864) ^ 64;
        else ab = ((ab + 36864) ^ 64) + dbuf;
        PIN(ab);
      }
      if constexpr (n%36==22) bias[0] = *(LAS const f32x4*)(size_t)(ba);
      if constexpr (n%36==24) bias[1] = *(LAS const f32x4*)(size_t)(ba+64);
      if constexpr (n%36==35) soff = ((6*P + 2*c.u + hp)*8 + 2*t)*1024;
      SB;
    });
    dbuf = -dbuf;
  }
  NOP8;
  p2_compute(c, [&](auto o_){ if constexpr (decltype(o_)::value<34) epi(IC(1), o_); });
}

constexpr int LEADA = 4;
DEVI void computeA1(const Ctx& c, bf16x8 (&xf)[2][18]){
  const int lane=c.lane, t=c.w, g=lane>>4, li=lane&15, q=(lane&15)>>2, p=lane&3;
  const u32 F = ((q>>1)&1) | ((g&1)<<1);
  const u32 lp = TOP - 36864 + (4*g+q)*128 + 32*F + 8*p;
  u32 ab[4] = {lp, lp^32, lp^64, lp^96};
  PIN(ab[0]); PIN(ab[1]); PIN(ab[2]); PIN(ab[3]);
  s16x4 lo[RING], hi[RING];
  f32x4 acc[4][2]; u32x2 pk[4][2];
  #pragma unroll
  for(int b=0;b<4;b++) for(int e=0;e<2;e++) asm volatile("" : "=v"(acc[b][e]));
  wait_vm<0>(); BAR;
  #pragma unroll
  for(int m=0;m<LEADA;m++){ lo[m]=ldstr(ab[m&3]+8*4096+2048); hi[m]=ldstr(ab[m&3]+8*4096); }
  u32 wl = (7+16*t+li)*ROWB + ((8*g) ^ (((7+li)&4)*4)), wa = wl; PIN(wl);
  u32 t7[7]; a_blocks(t7, c.i);
  u32 tp = 0x1a1a1a1au;
  auto cvt = [&](int b, int e){ u32x2 v = {pk2(acc[b][e][0],acc[b][e][1]), pk2(acc[b][e][2],acc[b][e][3])}; PIN(v); pk[b][e]=v; };
  #pragma unroll 1
  for(int P=0;P<7;P++){
    sfor<72>([&](auto n_){
      constexpr int n=decltype(n_)::value, s=n>>2, b=n&3, m=(n+LEADA)%72, ms=m>>2;
      const bf16x8 a = cat(lo[n%RING], hi[n%RING]);
      constexpr int e0 = b&1, e1 = 1-e0;
      if constexpr (s==0){ cvt(b,e0); mfma_zero(acc[b][e0], a, xf[e0][s]); } else mfma_acc(acc[b][e0], a, xf[e0][s]);
      SB;
      if constexpr (m%8==0) BAR;
      lo[(n+LEADA)%RING] = ldstr(ab[m&3] + (8-ms%9)*4096 + 2048);
      if constexpr (s==0) cvt(b,e1);
      SB;
      if constexpr (s==0) mfma_zero(acc[b][e1], a, xf[e1][s]); else mfma_acc(acc[b][e1], a, xf[e1][s]);
      SB;
      hi[(n+LEADA)%RING] = ldstr(ab[m&3] + (8-ms%9)*4096);
      if constexpr (n%12==4 && n<48){ wa = wl + ((tp >> (8*(n/12))) & 31)*32; PIN(wa); }
      if constexpr (n%6==5 && n<48){ constexpr int z=n/6; *(LAS u32x2*)(size_t)(wa + (z&1)*PARB) = pk[z>>1][z&1]; }
      if constexpr (n>=50 && n<57){ if constexpr (n==50) tp = t7[0]; else t7[n-51] = t7[n-50]; }
      SB;
    });
  }
  NOP8;
  #pragma unroll
  for(int z=0;z<8;z++){ const int b=z>>1, e=z&1;
    *(LAS u32x2*)(size_t)(wl + e*PARB + ((tp >> (8*b)) & 31)*32) = u32x2{pk2(acc[b][e][0],acc[b][e][1]), pk2(acc[b][e][2],acc[b][e][3])}; }
}

template<int L> DEVI void loaderB2(const Ctx& c, const u32 (&b3)[3]){
  u32 vo[7], vl[7]; p2_voff<L>(vo,vl,b3,c);
  P2L<L> ld; ld.init(c);
  wait_vm<19>(); BAR;
  ld.step(vo);
  wait_vm<13>(); BAR;
  ld.steps(vo,1);
  wait_vm<12>(); BAR;
  ld.steps(vo,1); BAR; BAR;
  ld.steps(vo,1);
  wait_vm<3*P2L<L>::N>(); BAR;
  ld.steps(vo,1);
  ld.run(vo,vl);
}
DEVI void loaderB(const Ctx& c, const u32 (&v)[4]){
  const int L = c.w-4;
  u32 b3[3];
  const char* src = c.w1 + (size_t)(1152*c.i + 64*c.u)*2 - 4096 + 384 + (size_t)(L-1)*16*9216;
  u32 dst = BUF1 + 73728 - 2048*(L+1);
  auto quarter = [&]{
    kpair(dst, src, v[0], v[1]); kpair(dst-6144, src + 48*9216, v[0], v[1]); kpair(dst-12288, src + 96*9216, v[0], v[1]);
    dst -= 18432; src += 144*9216;
  };
  u32 tid = ((c.r*3+c.u) + L*48)*27 + (c.lane>26? 26 : c.lane);
  const char* const w2s = c.w2 + (size_t)c.i*1152*1152; const char* const wos = c.wo + (size_t)c.i*144*1152 - 10368*128;
  const char* tp = w2s + tid*128;
  quarter(); wait_vm<7>(); BAR; quarter(); BAR; quarter();
  p2_base(b3, c);
  #pragma unroll 1
  for(int P=2;P<6;P++){
    if (P==2){ wait_vm<12>(); BAR; quarter(); src += 384 - 576*9216; dst += 147456; quarter(); wait_vm<12>(); BAR; quarter(); }
    else { src += 384 - 576*9216; dst += (P&1)? 0 : 147456;
      wait_vm<19>(); BAR; quarter(); quarter();
      wait_vm<19>(); BAR; }
    quarter();
    touch(SCR, tp);
    wait_vm<19>(); BAR; quarter();
    BAR;
    if (P==5) break;
    tid += 144*27; tp = tid>11663? src + 4096 - 64*9216 : (tid<10368? w2s : wos) + tid*128;
  }
  if (L==0) loaderB2<0>(c,b3); else if (L==1) loaderB2<1>(c,b3); else loaderB2<2>(c,b3);
}
DEVI void loaderA1(const Ctx& c, u32 (&v)[4], u32 (&dco)[6]){
  const int L = c.w-4;
  if (L==2){
    #pragma unroll 1
    for(int J=1;J<=41;J++) BAR;
    auto rp = __builtin_amdgcn_make_buffer_rsrc(c.ws + XCH_OFF + (size_t)(c.r*4)*4*36864, 0, 12*36864, 0x00020000);
    #pragma unroll 1
    for(int kb=0;kb<44;kb+=2){
      const u32 so = ((kb>=36? 108:0) + c.i*36 + kb)*1024;
      __builtin_amdgcn_raw_buffer_store_b128(u32x4{~0u,~0u,~0u,~0u}, rp, c.lane*16, so, 16);
      __builtin_amdgcn_raw_buffer_store_b128(u32x4{~0u,~0u,~0u,~0u}, rp, c.lane*16+1024, so, 16);
      BAR; }
  } else {
    u32 dst = L? TOP-4096 : TOP-9*4096;
    const char* src = c.wqkv - 4096 + (size_t)(8+L)*32*3456;
    auto one = [&]{
      wait_vm<8>(); BAR;
      kstep4(dst, src, v);
      src += 64*3456; dst = dst < TOP-7*4096 ? dst + 7*4096 : dst - 8192;
    };
    one(); one(); one(); one(); one();
    #pragma unroll 1
    for(int P=1;P<7;P++){
      src -= 576*3456;
      #pragma unroll
      for(int z=0;z<4;z++) v[z] += dco[0];
      one();
      #pragma unroll
      for(int z=0;z<5;z++) dco[z] = dco[z+1];
      one(); one(); one(); one(); one(); one(); one(); one();
    }
    wait_vm<8>(); BAR; wait_vm<4>(); BAR; wait_vm<0>(); BAR; BAR;
  }
}
DEVI void syncB(const Ctx& c, u64 tag){
  GAS u64* mine = synw(c,c.r,8+c.i*4+c.u);
  GAS u64* pf = synw(c, c.r, 8 + c.i*4 + (c.lane&3));
  #pragma unroll 1
  for(int z=0;z<12;z++) BAR;
  if (c.lane==0) __hip_atomic_store(mine, (tag<<3)|1, RLX_AGENT);
  BAR; BAR; BAR; BAR;
  u64 v = __hip_atomic_load(pf, RLX_AGENT);
  BAR; BAR;
  need(pf, v, (c.lane&3)<3, tag, 1);
  if (c.lane==0) __hip_atomic_store(mine, (tag<<3)|3, RLX_AGENT);
  BAR; BAR;
  {
    const int g = c.lane>>4;
    const u32 vo = (c.r*128 + (c.i>>1)*32 + 2*(c.lane&15) + (c.i&1))*1152 + (g&1)*32 + (g>>1)*16 + c.u*384;
    #pragma unroll 1
    for(int t=0;t<4;t++){ const char* xs = c.x + (t>>1)*64*1152 + (t&1)*192; glds16(t*3072, xs, vo); glds16(t*3072+1024, xs + 64, vo); glds16(t*3072+2048, xs + 128, vo); if (t==1) BAR; }
    glds16(BRAW, c.b2 + c.u*384, (c.lane<24? c.lane : 23)*16);
  }
  v = __hip_atomic_load(pf, RLX_AGENT);
  BAR; BAR; BAR;
  need(pf, v, (c.lane&3)<3, tag, 3);
  if (c.lane==0) __hip_atomic_store(mine, (tag<<3)|4, RLX_AGENT);
  p2_sync(c, tag);
}
DEVI void syncA1(const Ctx& c, u64 tag, u64 tagn){
  const int lane=c.lane;
  u32 la[8];
  #pragma unroll
  for(int z=0;z<8;z++){ int ci=z*64+lane; ci = ci>503? 503:ci; const int hr=ci/36; la[z] = (hr + (hr>=7? 64:0))*ROWB + (ci-36*hr)*16; PIN(la[z]); BAR; }
  #pragma unroll 1
  for(int J=9;J<=51;J++) BAR;
  { auto rs = __builtin_amdgcn_make_buffer_rsrc(c.ws + HALO_OFF + (size_t)(((c.r+1)&15)*4+c.i)*8192, 0, c.r<15? 8192:0, 0x00020000);
    #pragma unroll
    for(int z=0;z<8;z++){ __builtin_amdgcn_raw_buffer_store_b128(__builtin_bit_cast(u32x4, ldsb128(la[z] + 64*ROWB)), rs, lane*16, z*1024, 16); if (z==3) BAR; } }
  BAR; BAR; BAR;
  wait_vm<0>();
  if (c.r<15 && lane==0) __hip_atomic_store(synw(c,c.r+1,1+c.i), (tagn<<3)|1, RLX_AGENT);
  BAR; BAR; BAR;
  GAS u64* pf = synw(c, c.r, 1+c.i);
  u64 v = __hip_atomic_load(pf, RLX_AGENT);
  BAR; BAR;
  need(pf, v, c.r>0, tag, 1);
  __builtin_amdgcn_fence(__ATOMIC_ACQUIRE, "wavefront");
  u32x4 hv[8];
  { auto rs = __builtin_amdgcn_make_buffer_rsrc(c.ws + HALO_OFF + (size_t)(c.r*4+c.i)*8192, 0, c.r>0? 8192:0, 0x00020000);
    #pragma unroll
    for(int z=0;z<8;z++) hv[z] = __builtin_amdgcn_raw_buffer_load_b128(rs, lane*16, z*1024, 16); }
  BAR; BAR; BAR;
  #pragma unroll
  for(int z=0;z<8;z++) *(LAS u32x4*)(size_t)la[z] = hv[z];
  VM0_SEEN;
}

template<bool isA> DEVI void body(const Ctx& c){
  const char* x=c.x;
  const int w=c.w, lane=c.lane, bt=w&3, par=w>>2, g=lane>>4, li=lane&15;
  if (w==5){ u32 o = (((c.r&1)*4+c.u)*64 + lane)*128; o = o < c.clen-4 ? o : 0; glds4(SCR, c.code, o); }
  if (w==7){
    const u32 eo = (((lane&2)? (c.r+1)&15 : c.r)<<13) + (lane&1)*4;
    asm volatile("s_mov_b32 m0, %0\n\ts_nop 0\n\tglobal_load_lds_dword %1, %2 sc1" :: "s"(EPO), "v"(eo), "s"(c.ws + SYN_OFF) : "memory");
    if constexpr (!isA){ const int ln = lane<48? lane : 47; glds16(BRAW, c.b1, (1152*c.i + 64*c.u + 192*(ln>>3))*2 + (ln&7)*16); }
  }
  auto wline = [&](int row, int ln)->const char* {
    const char* a = c.w1 + (size_t)row*9216 + 2304*c.i; const char* b = c.wqkv + (size_t)row*3456 + ((288*c.i)&~127);
    return ln<6 ? a + ln*128 : ln<9 ? b + 1152 + (ln-6)*128 : ln<21 ? a + (ln-3)*128 : ln<24 ? b + 2304 + (ln-21)*128 : b + (ln-24)*128;
  };
  if (par){
    const int l2 = lane>17? 17:lane, h9 = l2>=9;
    const char* p = x + (c.r*128 + (c.i*4+c.u)*8 + bt*2 + h9)*1152 + (l2-9*h9)*128;
    touch(SCR, p);
  }
  const u32 XR = (par? 0 : 73728) + bt*18432;
  const u32 xv0 = (c.r*128 + 32*bt + 2*(lane>>3) + par)*1152 + (((lane&7) ^ ((lane>>3)&6))*16), xv1 = xv0 + 16*1152 - 1024;
  #pragma unroll
  for(int kc=0;kc<9;kc++) kpair(XR + kc*2048, x + kc*128, xv0, xv1);
  u32 wv[4];
  const char* wb;
  if constexpr (isA){ u32 t7[7]; a_blocks(t7, c.i); p1_vA(wv, lane, t7[0], c.i); wb = c.wqkv - 4096; }
  else { p1_vB(wv, lane); wb = c.w1 + (size_t)(1152*c.i + 64*c.u)*2 - 4096; }
  constexpr int KST = 32*(isA? 3456 : 9216);
  const char *tpa;
  { const int id = bt*54 + (lane>53? 53:lane), rr = id/24; tpa = wline((c.r*4+c.u)*9 + rr, 3 + id-24*rr); }
  bf16x8 xf[2][18];
  f32x4 sm={0,0,0,0}, sq={0,0,0,0};
  { const u32 ra = XR + (li>>3)*1024 + (li&7)*128 + ((g ^ (li&6))*16), rb = ra ^ 64;
    const u32x4 one = {0x3f803f80u,0x3f803f80u,0x3f803f80u,0x3f803f80u};
    const bf16x8 ones = __builtin_bit_cast(bf16x8, one);
    auto rd = [&](int a, int b){
      #pragma unroll
      for(int s=a;s<b;s++) xf[0][s] = ldsb128(((s&1)? rb:ra) + (s>>1)*2048);
      SB; };
    auto mm = [&](int a, int b){
      #pragma unroll
      for(int s=a;s<b;s++){ sm=MFMA(ones,xf[0][s],sm); sq=MFMA(xf[0][s],xf[0][s],sq); }
      SB; };
    wait_vm<6>(); rd(0,12); mm(0,8); wait_vm<4>(); rd(12,14); mm(8,12);
    wait_vm<0>();
    rd(14,18); mm(12,18);
  }
  auto wfirst = [&](int j){
    const int ks = j<4 ? (bt==0? (j==3? 17:j) : 15-4*(bt-1)-(bt==3? 1:0)-(3-j)) : (bt<2? 16:7);
    if (ks > (isA? 7 : 12)) return;
    if (j<4) kstep4(TOP - 4096*(ks+1), wb + (size_t)ks*KST, wv);
    else kpair(TOP - 4096*(ks+1) + (bt&1)*2048, wb + (size_t)ks*KST - (bt&1)*(KST/2), wv[0], wv[1]);
  };
  f32x2 rsm;
  {
    float d = (li&2)? ((li&1)? sq[3]:sq[2]) : ((li&1)? sq[1]:sq[0]);
    d = xsum(g==(li>>2)? d : 0.f);
    const float mean = sm[0]*(1.f/576.f);
    float var = __builtin_fmaf(d, 1.f/576.f, -mean*mean); var = var>0.f? var : 0.f;
    const float sd = rbf(__builtin_amdgcn_sqrtf(rbf(rbf(var)+1e-5f)));
    rsm = f32x2{__builtin_amdgcn_rcpf(sd), -rbf(mean)};
  }
  auto convert = [&](bf16x8 (&dst)[18], u32 xr, f32x2 st){
    constexpr int K0 = 0, K1 = 36;
    const u32 hb = pk2(st[0],st[0]) & 0xffffu; const float hf = lo2f(hb); const u32 lb = pk2(st[0]-hf, 0.f) & 0xffffu;
    const float c0 = st[1]*(hf + lo2f(lb));
    const bool on = g==(li>>2); const int sh = (li&1)*16;
    const u32 h1 = on? hb<<sh : 0u, l1 = on? lb<<sh : 0u;
    const bf16x8 dg = __builtin_bit_cast(bf16x8, u32x4{(li&2)? 0u:h1, (li&2)? h1:0u, (li&2)? 0u:l1, (li&2)? l1:0u});
    const f32x4 cc = {c0,c0,c0,c0};
    const int q=li>>2, p=lane&3;
    const u32 xa = xr + (4*g+q)*128 + 32*(((q>>1)&1) | ((g&1)<<1)) + 8*p;
    u32 xb[4] = {xa, xa^32, xa^64, xa^96};
    PIN(xb[0]); PIN(xb[1]); PIN(xb[2]); PIN(xb[3]);
    u32 xc[4] = {xb[0],xb[1],xb[2],xb[3]};
    PIN(xc[0]); PIN(xc[1]); PIN(xc[2]); PIN(xc[3]);
    constexpr int LDX = 6;
    s16x4 ta[36], tb2[36]; f32x4 dd[36];
    auto rdx = [&](auto k_){ constexpr int k=decltype(k_)::value+K0; if constexpr (k<K1){ ta[k] = ldstr(xb[k&3] + (k>>2)*2048); tb2[k] = ldstr(xc[k&3] + (k>>2)*2048); } };
    sfor<LDX>(rdx);
    sfor<K1-K0+2>([&](auto k_){ constexpr int k=decltype(k_)::value+K0;
      if constexpr (k<K1 && (k&1)==0 && k+LDX<=K1) __builtin_amdgcn_s_waitcnt(0xC07F | ((2*(LDX-2))<<8));
      if constexpr (k<K1) dd[k] = MFMA(cat(ta[k],tb2[k]), dg, cc);
      SB;
      rdx(std::integral_constant<int,k-K0+LDX>{});
      if constexpr (k>=K0+3 && (k&1)==1){ constexpr int z=k-3;
        dst[z>>1] = __builtin_bit_cast(bf16x8, u32x4{pk2(dd[z][0],dd[z][1]), pk2(dd[z][2],dd[z][3]), pk2(dd[z+1][0],dd[z+1][1]), pk2(dd[z+1][2],dd[z+1][3])}); PIN(dst[z>>1]); }
      SB;
    });
  };
  if (par){
    if (g==0) *(LAS f32x2*)(size_t)(STA + bt*128 + li*8) = rsm;
  } else {
    convert(xf[0], XR, rsm);
    wfirst(0); wfirst(1); wfirst(2); wfirst(3); wfirst(4);
  }
  LBAR;
  if (!par){ convert(xf[1], bt*18432, *(LAS const f32x2*)(size_t)(STA + bt*128 + li*8)); }
  else {
    if (!isA && w<7){
      kstep4(TOP - 4096*(w+10), wb + (size_t)(w+9)*KST, wv); if (w<6) kstep4(TOP - 4096*(w+13), wb + (size_t)(w+12)*KST, wv); }
    touch(SCR, tpa);
  }
  u64 tag=0, tagn=0;
  if (w==7){
    if constexpr (!isA){ if (lane<48){
      const u32x4 bv = *(LAS const u32x4*)(size_t)(BRAW + lane*16);
      *(LAS f32x4*)(size_t)(TOP + lane*32) = f32x4{lo2f(bv[0]),hi2f(bv[0]),lo2f(bv[1]),hi2f(bv[1])};
      *(LAS f32x4*)(size_t)(TOP + lane*32 + 16) = f32x4{lo2f(bv[2]),hi2f(bv[2]),lo2f(bv[3]),hi2f(bv[3])}; } }
    tag = (*(LAS const u64*)(size_t)EPO + 1) & ((1ull<<61)-1); tagn = (*(LAS const u64*)(size_t)(EPO+8) + 1) & ((1ull<<61)-1);
  }
  u32 dco[6] = {0,0,0,0,0,0};
  if (isA && w==6) att_tables(lane);
  else if (isA && par){ u32 t7[7]; a_blocks(t7, c.i);
    #pragma unroll
    for(int z=0;z<6;z++){ dco[z] = a_col(lane,t7[z+1]) - a_col(lane,t7[z]); PIN(dco[z]); } }
  if (par) LBAR;
  if constexpr (!isA){
    if (w<4){ computeB(c, xf); }
    else if (w==7) syncB(c, tag);
    else loaderB(c, wv);
    return;
  } else {
    if (w<4) computeA1(c, xf); else if (w==7) syncA1(c, tag, tagn); else loaderA1(c, wv, dco);
    LBAR;
    attention(c, 2*bt+par);
    wait_vm<3>(); BAR;
    if (w==7 && lane==0) __hip_atomic_store(synw(c,c.r,8+c.i*4+3), (tag<<3)|5, RLX_AGENT);
    if (w==7) wait_vm<1>(); else wait_vm<0>();
    BAR;
    if (w==7 && lane==0) __hip_atomic_store(synw(c,c.r,8+c.i*4+3), (tag<<3)|6, RLX_AGENT);
    #pragma unroll
    for(int z=0;z<8;z++){ const int kb = 44+z*8+w, u = kb/36;
      __builtin_amdgcn_raw_buffer_store_b128(u32x4{~0u,~0u,~0u,~0u}, xch_rsrc(c, u), lane*16, c.i*36864 + (kb-36*u)*1024, 16); }
    wait_vm<0>(); BAR;
    if (w==7 && lane==0) __hip_atomic_store(synw(c,c.r,8+c.i*4+3), (tag<<3)|7, RLX_AGENT);
  }
}

__global__ __launch_bounds__(512,2) void fused_kernel(const char* __restrict__ x, const char* __restrict__ wqkv, const char* __restrict__ wo,
    const char* __restrict__ w1, const char* __restrict__ b1, const char* __restrict__ w2, const char* __restrict__ b2, char* __restrict__ out, char* __restrict__ ws){
  Ctx c; c.x=x; c.wqkv=wqkv; c.wo=wo; c.w1=w1; c.b1=b1; c.w2=w2; c.b2=b2; c.out=out; c.ws=ws;
  const int bid = blockIdx.x, xcd = bid&7, idx = bid>>3;
  c.i = xcd>>1; c.r = (xcd&1)*8 + (idx>>2); c.u = idx&3;
  c.w = __builtin_amdgcn_readfirstlane(threadIdx.x>>6); c.lane = threadIdx.x&63;
  {
    u64 pc; u32 len;
    asm volatile("s_getpc_b64 %0\nLkbeg:\n\ts_mov_b32 %1, Lkend-Lkbeg\n\t.subsection 1\nLkend:\n\t.subsection 0" : "=s"(pc), "=s"(len));
    c.code = (const char*)pc; c.clen = len; }
  if (c.u==3) body<true>(c); else body<false>(c);
}

extern "C" void kernel_launch(void* const* d_in, const int* in_sizes, int n_in, void* d_out, int out_size, void* d_ws, size_t ws_size, hipStream_t stream){
  (void)hipFuncSetAttribute((const void*)fused_kernel, hipFuncAttributeMaxDynamicSharedMemorySize, LDS_BYTES);
  fused_kernel<<<256,512,LDS_BYTES,stream>>>((const char*)d_in[0],(const char*)d_in[1],(const char*)d_in[2],(const char*)d_in[4],(const char*)d_in[5],
      (const char*)d_in[6],(const char*)d_in[7],(char*)d_out,(char*)d_ws);
}
```
